```python
import torch

def _softmax(x, axis=-1):
    return torch.softmax(x, dim=axis)
def _gelu(x):
    return torch.nn.functional.gelu(x, approximate="tanh")
def _silu(x):
    return torch.nn.functional.silu(x)
def _sigmoid(x):
    return torch.sigmoid(x)
def _layernorm(x, axis=-1, eps=1e-5):
    m = x.mean(dim=axis, keepdim=True)
    v = x.var(dim=axis, keepdim=True, unbiased=False)
    return (x - m) / torch.sqrt(v + eps)
def _scatter_add(tgt, idx, src, axis=0):
    return tgt.clone().index_add_(axis, idx, src)
def _segment_sum(src, seg, n):
    out = torch.zeros((n,) + src.shape[1:], dtype=src.dtype)
    return out.index_add_(0, seg, src)
def _pad(x, pw):
    flat = tuple(v for pair in reversed(pw) for v in pair)
    return torch.nn.functional.pad(x, flat)

kernel_name = 'standalone__weight_only_int_gptq__prompt'

def setup_inputs():

    _w = (lambda a: (lambda t: t.to(torch.bfloat16) if t.is_floating_point() else t)(torch.as_tensor(a)))
    B, S, D, F = 1, 2048, 576, 4608
    bits = max(2, min(4, 3))
    pf = 32 // bits
    G = -1 if -1 > 0 else D
    G = max(pf, (G // pf) * pf)
    D = max(G, (D // G) * G)
    ng = D // G
    qmax = 1 << bits
    qw = torch.randint(0, qmax, (D, F)).to(torch.int64)
    packed = torch.zeros((D // pf, F), dtype=torch.int64)
    for i in range(pf):
        packed |= (qw[i::pf] << (i * bits))
    packed = packed.to(torch.int32)
    return {
        "x": _w((lambda *s: torch.randn(*s, dtype=torch.bfloat16) * 0.05)(B, S, D)),
        "qw_packed": _w(packed),
        "scales": _w(torch.abs((lambda *s: torch.randn(*s, dtype=torch.bfloat16) * 0.05)(ng, F)).to(float) * 0.1 + 0.01),
        "zeros": _w(torch.randint(0, qmax, (ng, F)).to(float)),
        "bits": _w((lambda _x: torch.as_tensor(_x, dtype=torch.int32))(bits)),
    }

def reference(x, qw_packed, scales, zeros, bits):
    b = int(bits)
    pf = 32 // b
    mask = (1 << b) - 1
    P, F = qw_packed.shape
    D = P * pf
    ng = scales.shape[0]
    G = D // ng
    shifts = (lambda x, dt: x.to(dt))(torch.arange(pf) * b, qw_packed.dtype)
    qw = ((qw_packed[:, None, :] >> shifts[None, :, None]) & mask).reshape(D, F)
    s = (lambda x, n, axis: torch.repeat_interleave(x, n, dim=axis))(scales, G, axis=0)
    z = (lambda x, n, axis: torch.repeat_interleave(x, n, dim=axis))(zeros, G, axis=0)
    w = ((lambda x, dt: x.to(dt))(qw, x.dtype) - z) * s
    return torch.einsum("bsd,df->bsf", x, w)
```

```cpp
#include <hip/hip_runtime.h>
#include <hip/hip_bf16.h>
#include <stdint.h>


using bf16x8 = __attribute__((ext_vector_type(8))) short;
using f32x4  = __attribute__((ext_vector_type(4))) float;

namespace {

constexpr int M_ = 2048, K_ = 570, N_ = 4608, P_ = 57;
constexpr int BITS = 3, PF = 10;
constexpr int BM = 256, BN = 144;
constexpr int NT = BN / 16;
constexpr int CW = 16;
constexpr int PITCH = 352;
constexpr int BUFB = BN * PITCH;

struct __attribute__((packed, aligned(4))) u4 { uint32_t a, b, c, d; };

typedef __attribute__((ext_vector_type(2))) __bf16 bf16x2_t;
__device__ __forceinline__ uint32_t pk_bf16(float lo, float hi) {
    bf16x2_t v = {(__bf16)lo, (__bf16)hi};
    return __builtin_bit_cast(uint32_t, v);
}
__device__ __forceinline__ float bf2f(uint16_t v) { return __uint_as_float((uint32_t)v << 16); }

__device__ __forceinline__ bf16x8 load_xfrag(const uint16_t* __restrict__ x, int row, int k) {
    u4 v;
    const uint16_t* p = x + (size_t)row * K_ + k;
    if (k + 8 <= K_) {
        v = *(const u4*)p;
    } else {
        v.a = (k + 2 <= K_) ? *(const uint32_t*)p : 0u;
        v.b = (k + 4 <= K_) ? *(const uint32_t*)(p + 2) : 0u;
        v.c = (k + 6 <= K_) ? *(const uint32_t*)(p + 4) : 0u;
        v.d = 0u;
    }
    return __builtin_bit_cast(bf16x8, v);
}

__device__ __forceinline__ void dequant_store(unsigned char* dst, const uint32_t (&wq)[4], int p0,
                                              float s, float nzs) {
    uint32_t o[20];
#pragma unroll
    for (int i = 0; i < 4; ++i) {
        const bool valid = (p0 + i) < P_;
#pragma unroll
        for (int j = 0; j < 5; ++j) {
            float q0 = (float)((wq[i] >> (BITS * (2 * j))) & 7u);
            float q1 = (float)((wq[i] >> (BITS * (2 * j + 1))) & 7u);
            uint32_t v = pk_bf16(__builtin_fmaf(q0, s, nzs), __builtin_fmaf(q1, s, nzs));
            o[i * 5 + j] = valid ? v : 0u;
        }
    }
#pragma unroll
    for (int i = 0; i < 5; ++i)
        *(uint4*)(dst + 16 * i) = make_uint4(o[4 * i], o[4 * i + 1], o[4 * i + 2], o[4 * i + 3]);
}

__global__ __launch_bounds__(512, 2)
void gptq_gemm_kernel(const uint16_t* __restrict__ x, const uint32_t* __restrict__ qw,
                      const uint16_t* __restrict__ scales, const uint16_t* __restrict__ zeros,
                      uint16_t* __restrict__ out) {
    __shared__ __attribute__((aligned(16))) unsigned char lds[2 * BUFB];

    const int tid = threadIdx.x;
    const int wave = tid >> 6, lane = tid & 63;
    const int l15 = lane & 15, q = lane >> 4;
    const int mb = blockIdx.x & 7, nb = blockIdx.x >> 3;
    const int m0 = mb * BM + wave * 32;
    const int n0 = nb * BN;

    const int colA = n0 + wave * 16 + l15;
    const int colB = n0 + 8 * 16 + l15;
    const float sA = bf2f(scales[colA]), nzA = -bf2f(zeros[colA]) * sA;
    const float sB = bf2f(scales[colB]), nzB = -bf2f(zeros[colB]) * sB;

    f32x4 acc[2][NT];
#pragma unroll
    for (int a = 0; a < 2; ++a)
#pragma unroll
        for (int b = 0; b < NT; ++b) acc[a][b] = f32x4{0.f, 0.f, 0.f, 0.f};

    uint32_t wqA[4], wqB[4];
    bf16x8 xf[2][5][2];

    auto load_words = [&](int c) {
#pragma unroll
        for (int i = 0; i < 4; ++i) {
            const int p = c * CW + q * 4 + i;
            wqA[i] = p < P_ ? qw[(size_t)p * N_ + colA] : 0u;
        }
        if (wave == c) {
#pragma unroll
            for (int i = 0; i < 4; ++i) {
                const int p = c * CW + q * 4 + i;
                wqB[i] = p < P_ ? qw[(size_t)p * N_ + colB] : 0u;
            }
        }
    };
    auto dequant = [&](int c) {
        unsigned char* buf = lds + (c & 1) * BUFB;
        const int p0 = c * CW + q * 4;
        dequant_store(buf + (wave * 16 + l15) * PITCH + q * 80, wqA, p0, sA, nzA);
        if (wave == c) dequant_store(buf + (128 + l15) * PITCH + q * 80, wqB, p0, sB, nzB);
    };

    load_words(0);
#pragma unroll
    for (int t = 0; t < 5; ++t)
#pragma unroll
        for (int s = 0; s < 2; ++s) xf[0][t][s] = load_xfrag(x, m0 + s * 16 + l15, t * 32 + q * 8);
    dequant(0);
    __syncthreads();

#pragma unroll
    for (int c = 0; c < 4; ++c) {
        const int steps = (c < 3) ? 5 : 3;
        if (c < 3) {
            load_words(c + 1);
            const int nsteps = (c + 1 < 3) ? 5 : 3;
#pragma unroll
            for (int t = 0; t < 5; ++t)
                if (t < nsteps)
#pragma unroll
                    for (int s = 0; s < 2; ++s)
                        xf[(c + 1) & 1][t][s] =
                            load_xfrag(x, m0 + s * 16 + l15, (c + 1) * 160 + t * 32 + q * 8);
        }
        const unsigned char* buf = lds + (c & 1) * BUFB;
#pragma unroll
        for (int t = 0; t < 5; ++t) {
            if (t < steps) {
#pragma unroll
                for (int b = 0; b < NT; ++b) {
                    bf16x8 wf = *(const bf16x8*)(buf + (b * 16 + l15) * PITCH + t * 64 + q * 16);
#pragma unroll
                    for (int s = 0; s < 2; ++s)
                        acc[s][b] = __builtin_amdgcn_mfma_f32_16x16x32_bf16(wf, xf[c & 1][t][s],
                                                                            acc[s][b], 0, 0, 0);
                }
            }
        }
        if (c < 3) {
            dequant(c + 1);
            __syncthreads();
        }
    }

#pragma unroll
    for (int s = 0; s < 2; ++s) {
        uint16_t* orow = out + (size_t)(m0 + s * 16 + l15) * N_ + n0 + 4 * q;
#pragma unroll
        for (int b = 0; b < NT; ++b) {
            uint2 v;
            v.x = pk_bf16(acc[s][b][0], acc[s][b][1]);
            v.y = pk_bf16(acc[s][b][2], acc[s][b][3]);
            *(uint2*)(orow + b * 16) = v;
        }
    }
}

}

extern "C" void kernel_launch(void* const* d_in, const int* in_sizes, int n_in,
                              void* d_out, int out_size, void* d_ws, size_t ws_size,
                              hipStream_t stream) {
    const uint16_t* x = (const uint16_t*)d_in[0];
    const uint32_t* qw = (const uint32_t*)d_in[1];
    const uint16_t* scales = (const uint16_t*)d_in[2];
    const uint16_t* zeros = (const uint16_t*)d_in[3];
    uint16_t* out = (uint16_t*)d_out;
    gptq_gemm_kernel<<<(M_ / BM) * (N_ / BN), 512, 0, stream>>>(x, qw, scales, zeros, out);
}
```

```cpp
#include <hip/hip_runtime.h>
#include <hip/hip_bf16.h>
#include <stdint.h>


using bf16x8 = __attribute__((ext_vector_type(8))) short;
using f32x4  = __attribute__((ext_vector_type(4))) float;

namespace {

constexpr int M_ = 2048, K_ = 570, N_ = 4608, P_ = 57;
constexpr int BITS = 3, PF = 10;
constexpr int BM = 256, BN = 144;
constexpr int NT = BN / 16;
constexpr int CW = 16;
constexpr int PITCH = 352;
constexpr int BUFB = BN * PITCH;

struct __attribute__((packed, aligned(4))) u4 { uint32_t a, b, c, d; };

typedef __attribute__((ext_vector_type(2))) __bf16 bf16x2_t;
__device__ __forceinline__ uint32_t pk_bf16(float lo, float hi) {
    bf16x2_t v = {(__bf16)lo, (__bf16)hi};
    return __builtin_bit_cast(uint32_t, v);
}
__device__ __forceinline__ float bf2f(uint16_t v) { return __uint_as_float((uint32_t)v << 16); }

__device__ __forceinline__ bf16x8 load_xfrag(const uint16_t* __restrict__ x, int row, int k) {
    u4 v;
    const uint16_t* p = x + (size_t)row * K_ + k;
    if (k + 8 <= K_) {
        v = *(const u4*)p;
    } else {
        v.a = (k + 2 <= K_) ? *(const uint32_t*)p : 0u;
        v.b = (k + 4 <= K_) ? *(const uint32_t*)(p + 2) : 0u;
        v.c = (k + 6 <= K_) ? *(const uint32_t*)(p + 4) : 0u;
        v.d = 0u;
    }
    return __builtin_bit_cast(bf16x8, v);
}

__device__ __forceinline__ void dequant_store(unsigned char* dst, const uint32_t (&wq)[4], int p0,
                                              float s, float nzs) {
    uint32_t o[20];
#pragma unroll
    for (int i = 0; i < 4; ++i) {
        const bool valid = (p0 + i) < P_;
#pragma unroll
        for (int j = 0; j < 5; ++j) {
            float q0 = (float)((wq[i] >> (BITS * (2 * j))) & 7u);
            float q1 = (float)((wq[i] >> (BITS * (2 * j + 1))) & 7u);
            uint32_t v = pk_bf16(__builtin_fmaf(q0, s, nzs), __builtin_fmaf(q1, s, nzs));
            o[i * 5 + j] = valid ? v : 0u;
        }
    }
#pragma unroll
    for (int i = 0; i < 5; ++i)
        *(uint4*)(dst + 16 * i) = make_uint4(o[4 * i], o[4 * i + 1], o[4 * i + 2], o[4 * i + 3]);
}

__global__ __launch_bounds__(512, 2)
void gptq_gemm_kernel(const uint16_t* __restrict__ x, const uint32_t* __restrict__ qw,
                      const uint16_t* __restrict__ scales, const uint16_t* __restrict__ zeros,
                      uint16_t* __restrict__ out) {
    __shared__ __attribute__((aligned(16))) unsigned char lds[2 * BUFB];

    const int tid = threadIdx.x;
    const int wave = tid >> 6, lane = tid & 63;
    const int l15 = lane & 15, q = lane >> 4;
    const int mb = blockIdx.x & 7, nb = blockIdx.x >> 3;
    const int m0 = mb * BM + wave * 32;
    const int n0 = nb * BN;

    const int colA = n0 + wave * 16 + l15;
    const int colB = n0 + 8 * 16 + l15;
    const float sA = bf2f(scales[colA]), nzA = -bf2f(zeros[colA]) * sA;
    const float sB = bf2f(scales[colB]), nzB = -bf2f(zeros[colB]) * sB;

    f32x4 acc[2][NT];
#pragma unroll
    for (int a = 0; a < 2; ++a)
#pragma unroll
        for (int b = 0; b < NT; ++b) acc[a][b] = f32x4{0.f, 0.f, 0.f, 0.f};

    uint32_t wA[4][4], wB[4];
    bf16x8 xf[2][5][2];

#pragma unroll
    for (int c = 0; c < 4; ++c)
#pragma unroll
        for (int i = 0; i < 4; ++i) {
            const int p = c * CW + q * 4 + i;
            wA[c][i] = p < P_ ? qw[(size_t)p * N_ + colA] : 0u;
        }
#pragma unroll
    for (int i = 0; i < 4; ++i) {
        const int p = wave * CW + q * 4 + i;
        wB[i] = (wave < 4 && p < P_) ? qw[(size_t)p * N_ + colB] : 0u;
    }
    auto dequant = [&](int c) {
        unsigned char* buf = lds + (c & 1) * BUFB;
        const int p0 = c * CW + q * 4;
        dequant_store(buf + (wave * 16 + l15) * PITCH + q * 80, wA[c], p0, sA, nzA);
        if (wave == c) dequant_store(buf + (128 + l15) * PITCH + q * 80, wB, p0, sB, nzB);
    };

#pragma unroll
    for (int t = 0; t < 5; ++t)
#pragma unroll
        for (int s = 0; s < 2; ++s) xf[0][t][s] = load_xfrag(x, m0 + s * 16 + l15, t * 32 + q * 8);
    dequant(0);
    __syncthreads();

#pragma unroll
    for (int c = 0; c < 4; ++c) {
        const int steps = (c < 3) ? 5 : 3;
        if (c < 3) {
            const int nsteps = (c + 1 < 3) ? 5 : 3;
#pragma unroll
            for (int t = 0; t < 5; ++t)
                if (t < nsteps)
#pragma unroll
                    for (int s = 0; s < 2; ++s)
                        xf[(c + 1) & 1][t][s] =
                            load_xfrag(x, m0 + s * 16 + l15, (c + 1) * 160 + t * 32 + q * 8);
        }
        const unsigned char* buf = lds + (c & 1) * BUFB;
        {
            constexpr int PFD = 3;
            const int nI = steps * NT;
            bf16x8 wf[PFD];
#pragma unroll
            for (int i = 0; i < PFD; ++i)
                if (i < nI) wf[i] = *(const bf16x8*)(buf + ((i % NT) * 16 + l15) * PITCH + (i / NT) * 64 + q * 16);
#pragma unroll
            for (int i = 0; i < nI; ++i) {
                const int t = i / NT, b = i % NT;
                const bf16x8 cur = wf[i % PFD];
                if (i + PFD < nI)
                    wf[i % PFD] = *(const bf16x8*)(buf + (((i + PFD) % NT) * 16 + l15) * PITCH + ((i + PFD) / NT) * 64 + q * 16);
#pragma unroll
                for (int s = 0; s < 2; ++s)
                    acc[s][b] = __builtin_amdgcn_mfma_f32_16x16x32_bf16(cur, xf[c & 1][t][s], acc[s][b], 0, 0, 0);
            }
        }
        if (c < 3) {
            dequant(c + 1);
            __syncthreads();
        }
    }

#pragma unroll
    for (int s = 0; s < 2; ++s) {
        uint16_t* orow = out + (size_t)(m0 + s * 16 + l15) * N_ + n0;
#pragma unroll
        for (int b = 0; b + 1 < NT; b += 2) {
            uint32_t a0 = pk_bf16(acc[s][b][0], acc[s][b][1]);
            uint32_t a1 = pk_bf16(acc[s][b][2], acc[s][b][3]);
            uint32_t c0 = pk_bf16(acc[s][b + 1][0], acc[s][b + 1][1]);
            uint32_t c1 = pk_bf16(acc[s][b + 1][2], acc[s][b + 1][3]);
            auto r0 = __builtin_amdgcn_permlane16_swap(a0, c0, false, false);
            auto r1 = __builtin_amdgcn_permlane16_swap(a1, c1, false, false);
            uint4 v = make_uint4(r0[0], r1[0], r0[1], r1[1]);
            const int qe = (q & 1);
            const int col = b * 16 + (qe ? 16 : 0) + (q >> 1) * 8;
            *(uint4*)(orow + col) = v;
        }
        {
            const int b = NT - 1;
            uint2 v;
            v.x = pk_bf16(acc[s][b][0], acc[s][b][1]);
            v.y = pk_bf16(acc[s][b][2], acc[s][b][3]);
            *(uint2*)(orow + b * 16 + 4 * q) = v;
        }
    }
}

}

extern "C" void kernel_launch(void* const* d_in, const int* in_sizes, int n_in,
                              void* d_out, int out_size, void* d_ws, size_t ws_size,
                              hipStream_t stream) {
    const uint16_t* x = (const uint16_t*)d_in[0];
    const uint32_t* qw = (const uint32_t*)d_in[1];
    const uint16_t* scales = (const uint16_t*)d_in[2];
    const uint16_t* zeros = (const uint16_t*)d_in[3];
    uint16_t* out = (uint16_t*)d_out;
    gptq_gemm_kernel<<<(M_ / BM) * (N_ / BN), 512, 0, stream>>>(x, qw, scales, zeros, out);
}
```
